```python
import torch

def _softmax(x, axis=-1):
    return torch.softmax(x, dim=axis)
def _gelu(x):
    return torch.nn.functional.gelu(x, approximate="tanh")
def _silu(x):
    return torch.nn.functional.silu(x)
def _sigmoid(x):
    return torch.sigmoid(x)
def _layernorm(x, axis=-1, eps=1e-5):
    m = x.mean(dim=axis, keepdim=True)
    v = x.var(dim=axis, keepdim=True, unbiased=False)
    return (x - m) / torch.sqrt(v + eps)
def _scatter_add(tgt, idx, src, axis=0):
    return tgt.clone().index_add_(axis, idx, src)
def _segment_sum(src, seg, n):
    out = torch.zeros((n,) + src.shape[1:], dtype=src.dtype)
    return out.index_add_(0, seg, src)
def _pad(x, pw):
    flat = tuple(v for pair in reversed(pw) for v in pair)
    return torch.nn.functional.pad(x, flat)

kernel_name = 'standalone__low_rank_proj_linformer__prompt'

def setup_inputs():

    _w = (lambda a: (lambda t: t.to(torch.bfloat16) if t.is_floating_point() else t)(torch.as_tensor(a)))
    B, H, Sq, Skv, Dh, R = 1, 16, 2048, 2048, 256, 16
    return {
        "q": _w((lambda *s: torch.randn(*s, dtype=torch.bfloat16) * 0.8)(B, H, Sq, Dh)),
        "k": _w((lambda *s: torch.randn(*s, dtype=torch.bfloat16) * 0.8)(B, H, Skv, Dh)),
        "v": _w((lambda *s: torch.randn(*s, dtype=torch.bfloat16) * 0.05)(B, H, Skv, Dh)),
        "e_proj": _w((lambda *s: torch.randn(*s, dtype=torch.bfloat16) * 0.8)(R, Skv) * (Skv ** -0.5)),
        "f_proj": _w((lambda *s: torch.randn(*s, dtype=torch.bfloat16) * 0.05)(R, Skv) * (Skv ** -0.5)),
    }

def reference(q, k, v, e_proj, f_proj):
    B, H, Sq, Dh = q.shape
    kp = torch.einsum("rn,bhnd->bhrd", e_proj, k)
    vp = torch.einsum("rn,bhnd->bhrd", f_proj, v)
    scores = torch.einsum("bhqd,bhrd->bhqr", q, kp) * (Dh ** -0.5)
    p = _softmax(scores, axis=-1)
    o = torch.einsum("bhqr,bhrd->bhqd", p, vp)
    return o
```

```cpp
#include <hip/hip_runtime.h>
#include <hip/hip_bf16.h>
#include <stdint.h>
#include <utility>

typedef unsigned int u32;
typedef unsigned long long u64;
typedef __attribute__((ext_vector_type(4))) u32 u32x4;
typedef __attribute__((ext_vector_type(2))) u32 u32x2;
typedef __attribute__((ext_vector_type(4))) float f32x4;
typedef __attribute__((ext_vector_type(2))) float f32x2;
typedef __attribute__((ext_vector_type(8))) __bf16 bf16x8;
typedef __attribute__((ext_vector_type(2))) __bf16 bf16x2;
typedef __attribute__((ext_vector_type(4))) short s16x4;
#define LAS __attribute__((address_space(3)))
#define GAS __attribute__((address_space(1)))

constexpr int kSeq = 2048, kDh = 256;
constexpr int kRowB = kDh * 2;
constexpr int kProjB = kSeq * 2;
constexpr u32 kLdsX = 0;
constexpr u32 kLdsE = 131072;
constexpr u32 kLdsKp = 131072;
constexpr u32 kLdsVp = 131072 + 8192;
constexpr u32 kLdsBytes = 131072 + 32768;
constexpr u32 kWsEpoch = 0;
constexpr u32 kWsFlag = 4096;
constexpr u32 kWsPay = 65536;
constexpr u32 kPayB = 2048;
constexpr u32 kUnwritten = 0xffffffffu;
constexpr u32 kSpinCap = 1u << 16;
constexpr u32 kScalarPolls = 24;
constexpr u64 kEarlyQTicks = 550;
constexpr int kPoller = 4;
constexpr int kEarlyQ = 6;

__device__ __forceinline__ u32 pack_bf16(float a, float b) {
    f32x2 x = {a, b};
    return __builtin_bit_cast(u32, __builtin_convertvector(x, bf16x2));
}
__device__ __forceinline__ float lo_f32(u32 x) { return __uint_as_float(x << 16); }
__device__ __forceinline__ float hi_f32(u32 x) { return __uint_as_float(x & 0xffff0000u); }
template <int N>
__device__ __forceinline__ void wait_vm() { asm volatile("s_waitcnt vmcnt(%0)" ::"i"(N) : "memory"); }
__device__ __forceinline__ void lds_barrier() { asm volatile("s_waitcnt lgkmcnt(0)\n\ts_barrier" ::: "memory"); }
__device__ __forceinline__ void raw_barrier() { asm volatile("s_barrier" ::: "memory"); }
__device__ __forceinline__ u64 rt() { return __builtin_amdgcn_s_memrealtime(); }
template <class T>
__device__ __forceinline__ void pin_v(T& x) { asm volatile("" : "+v"(x)); }
template <class T>
__device__ __forceinline__ void pin_s(T& x) { asm volatile("" : "+s"(x)); }
template <class T>
__device__ __forceinline__ T* uni(T* p) {
    const u64 a = (u64)p;
    const u32 lo = __builtin_amdgcn_readfirstlane((u32)a), hi = __builtin_amdgcn_readfirstlane((u32)(a >> 32));
    return (T*)(((u64)hi << 32) | lo);
}
template <class T>
__device__ __forceinline__ T lds_ld(u32 addr) { return *(LAS T*)(size_t)addr; }
template <class T>
__device__ __forceinline__ void lds_st(u32 addr, T v) { *(LAS T*)(size_t)addr = v; }
__device__ __forceinline__ s16x4 tr_read(u32 addr) {
    return __builtin_amdgcn_ds_read_tr16_b64_v4i16((LAS s16x4*)(size_t)addr);
}
__device__ __forceinline__ bf16x8 join8(s16x4 a, s16x4 b) {
    return __builtin_bit_cast(bf16x8, __builtin_shufflevector(a, b, 0, 1, 2, 3, 4, 5, 6, 7));
}
__device__ __forceinline__ float xor16_max(float x) {
    u32x2 r = __builtin_amdgcn_permlane16_swap(__float_as_uint(x), __float_as_uint(x), false, false);
    return fmaxf(__uint_as_float(r[0]), __uint_as_float(r[1]));
}
__device__ __forceinline__ float xor32_max(float x) {
    u32x2 r = __builtin_amdgcn_permlane32_swap(__float_as_uint(x), __float_as_uint(x), false, false);
    return fmaxf(__uint_as_float(r[0]), __uint_as_float(r[1]));
}
__device__ __forceinline__ float xor16_sum(float x) {
    u32x2 r = __builtin_amdgcn_permlane16_swap(__float_as_uint(x), __float_as_uint(x), false, false);
    return __uint_as_float(r[0]) + __uint_as_float(r[1]);
}
__device__ __forceinline__ float xor32_sum(float x) {
    u32x2 r = __builtin_amdgcn_permlane32_swap(__float_as_uint(x), __float_as_uint(x), false, false);
    return __uint_as_float(r[0]) + __uint_as_float(r[1]);
}
template <int S>
struct Idx { static constexpr int v = S; };
template <class F, int... I>
__device__ __forceinline__ void unroll_impl(F&& f, std::integer_sequence<int, I...>) { (f(Idx<I>{}), ...); }
template <int N, class F>
__device__ __forceinline__ void unroll(F&& f) { unroll_impl(f, std::make_integer_sequence<int, N>{}); }

template <int N>
__device__ __forceinline__ void dma_x(u32 lds, const char* base, u32& va, u32& vb) {
#define LIN_X2                                                   \
    "global_load_lds_dwordx4 %[va], %[b] nt\n\t"                \
    "s_add_u32 m0, m0, 0x400\n\t"                                \
    "v_add_u32 %[va], 0x2000, %[va]\n\t"                         \
    "global_load_lds_dwordx4 %[vb], %[b] nt\n\t"                \
    "s_add_u32 m0, m0, 0x400\n\t"                                \
    "v_add_u32 %[vb], 0x2000, %[vb]\n\t"
    if constexpr (N == 4)
        asm volatile("s_mov_b32 m0, %[l]\n\ts_nop 0\n\t" LIN_X2 LIN_X2
                     : [va] "+v"(va), [vb] "+v"(vb) : [l] "s"(lds), [b] "s"(base) : "scc", "memory");
    else
        asm volatile("s_mov_b32 m0, %[l]\n\ts_nop 0\n\t" LIN_X2 LIN_X2 LIN_X2 LIN_X2 LIN_X2 LIN_X2
                     : [va] "+v"(va), [vb] "+v"(vb) : [l] "s"(lds), [b] "s"(base) : "scc", "memory");
#undef LIN_X2
}
__device__ __forceinline__ void dma_e(u32 lds, const char* base, u32 v0, u32 v1, u32 v2, u32 v3) {
    asm volatile(
        "s_mov_b32 m0, %[l]\n\t"
        "s_nop 0\n\t"
        "global_load_lds_dwordx4 %[v0], %[b]\n\t"
        "s_add_u32 m0, m0, 0x400\n\t"
        "s_nop 0\n\t"
        "global_load_lds_dwordx4 %[v1], %[b]\n\t"
        "s_add_u32 m0, m0, 0x400\n\t"
        "s_nop 0\n\t"
        "global_load_lds_dwordx4 %[v2], %[b]\n\t"
        "s_add_u32 m0, m0, 0x400\n\t"
        "s_nop 0\n\t"
        "global_load_lds_dwordx4 %[v3], %[b]"
        :: [l] "s"(lds), [b] "s"(base), [v0] "v"(v0), [v1] "v"(v1), [v2] "v"(v2), [v3] "v"(v3) : "scc", "memory");
}
__device__ __forceinline__ void dma_q(u32 lds, const char* base, u32 q0, u32 half = 4096) {
    u32 t;
    asm volatile(
        "s_mov_b32 m0, %[l]\n\t"
        "v_xor_b32 %[t], 0x20, %[q]\n\t"
        "global_load_lds_dwordx4 %[q], %[b] nt\n\t"
        "global_load_lds_dwordx4 %[t], %[b] offset:1024 nt\n\t"
        "v_xor_b32 %[t], 0x40, %[q]\n\t"
        "global_load_lds_dwordx4 %[t], %[b] offset:2048 nt\n\t"
        "v_xor_b32 %[t], 0x60, %[q]\n\t"
        "global_load_lds_dwordx4 %[t], %[b] offset:3072 nt\n\t"
        "s_add_u32 m0, m0, %[h]\n\t"
        "v_xor_b32 %[t], 0x1080, %[q]\n\t"
        "global_load_lds_dwordx4 %[t], %[b] nt\n\t"
        "v_xor_b32 %[t], 0x10a0, %[q]\n\t"
        "global_load_lds_dwordx4 %[t], %[b] offset:1024 nt\n\t"
        "v_xor_b32 %[t], 0x10c0, %[q]\n\t"
        "global_load_lds_dwordx4 %[t], %[b] offset:2048 nt\n\t"
        "v_xor_b32 %[t], 0x10e0, %[q]\n\t"
        "global_load_lds_dwordx4 %[t], %[b] offset:3072 nt"
        : [t] "=&v"(t) : [l] "s"(lds), [b] "s"(base), [q] "v"(q0), [h] "s"(half) : "scc", "memory");
}
template <int OFF>
__device__ __forceinline__ void store_out(char* base, u32 voff, u32x4 o) {
    asm volatile("global_store_dwordx4 %0, %1, %2 offset:%3 nt\n\ts_nop 1" ::"v"(voff), "v"(o), "s"(base), "i"(OFF) : "memory");
}

struct Ctx {
    const char* qbase;
    char* obase;
    char* obase2;
    const char* gsrc;
    u32 ldsw;
    u32 q0;
    u32 goff;
    u32 kimg, vimg;
    u32 qfrag;
    u32 qfrag2;
    u32 fbase;
    u32 ooff;
};

template <int TS>
__device__ __forceinline__ void project(const u32 (&tra)[4], const u32 era, const u32 ldsw, const int lane) {
    f32x4 acc[4];
#pragma unroll
    for (int ct = 0; ct < 4; ++ct) acc[ct] = f32x4{0.f, 0.f, 0.f, 0.f};
    unroll<4>([&](auto s_) {
        constexpr int s = decltype(s_)::v;
        wait_vm<12 - 4 * s>();
        const bf16x8 e = lds_ld<bf16x8>(era ^ (s << 6));
        s16x4 xa[4], xb[4];
#pragma unroll
        for (int ct = 0; ct < 4; ++ct) {
            xa[ct] = tr_read(tra[ct] + s * 4096);
            xb[ct] = tr_read(tra[ct] + s * 4096 + 512);
        }
        __builtin_amdgcn_sched_barrier(0);
#pragma unroll
        for (int ct = 0; ct < 4; ++ct) {
            const bf16x8 x = join8(xa[ct], xb[ct]);
            if constexpr (TS == 0) acc[ct] = __builtin_amdgcn_mfma_f32_16x16x32_bf16(x, e, acc[ct], 0, 0, 0);
            else acc[ct] = __builtin_amdgcn_mfma_f32_16x16x32_bf16(e, x, acc[ct], 0, 0, 0);
        }
        __builtin_amdgcn_sched_barrier(0);
    });
#pragma unroll
    for (int ct = 0; ct < 4; ++ct) lds_st<f32x4>(ldsw + 3 * 4096 + ct * 1024 + lane * 16, acc[ct]);
}

template <int WC>
__device__ __forceinline__ void attend(Ctx& c, const int ntile) {
    {
        u32x4 G0, G1, G2, G3;
        const u32 off0 = c.goff, off1 = c.goff + 4 * kPayB;
        const char *s0 = c.gsrc, *s1 = c.gsrc + 8 * kPayB;
#define LIN_LD(R, S, O, F) "global_load_dwordx4 v[" #R "], %[off" #O "], %[" #S "]" F "\n\t"
        asm volatile(
            LIN_LD(112:115, s0, 0, " sc1") LIN_LD(116:119, s0, 1, " sc1")
            LIN_LD(120:123, s1, 0, " sc1") LIN_LD(124:127, s1, 1, " sc1")
            "s_barrier"
            : "={v[112:115]}"(G0), "={v[116:119]}"(G1), "={v[120:123]}"(G2), "={v[124:127]}"(G3)
            : [off0] "v"(off0), [off1] "v"(off1), [s0] "s"(s0), [s1] "s"(s1)
            : "memory");
        if constexpr (WC == 1) dma_q(c.ldsw, c.qbase, c.q0);
#define LIN_CHK(M, A0, A1, A2, A3, B0, B1, B2, B3)                 \
    "v_max3_u32 %[tmp], v" #A0 ", v" #A1 ", v" #A2 "\n\t"          \
    "v_max3_u32 %[tmp], %[tmp], v" #A3 ", v" #B0 "\n\t"            \
    "v_max3_u32 %[tmp], %[tmp], v" #B1 ", v" #B2 "\n\t"            \
    "v_max_u32 %[tmp], %[tmp], v" #B3 "\n\t"                       \
    "v_cmp_eq_u32_e64 %[" #M "], -1, %[tmp]\n\t"
        u32 cnt, tmp;
        u64 sv, m0, m1;
        asm volatile(
            "s_mov_b64 %[sv], exec\n\t"
            "s_mov_b32 %[cnt], 0\n\t"
            "s_waitcnt vmcnt(%[n0])\n"
            "1:\n\t"
            LIN_CHK(m0, 112, 113, 114, 115, 116, 117, 118, 119)
            LIN_CHK(m1, 120, 121, 122, 123, 124, 125, 126, 127)
            "s_or_b64 vcc, %[m0], %[m1]\n\t"
            "s_cbranch_scc0 6f\n\t"
            "s_add_u32 %[cnt], %[cnt], 1\n\t"
            "s_cmp_gt_u32 %[cnt], %[cap]\n\t"
            "s_cbranch_scc1 6f\n\t"
            "s_sleep 1\n\t"
            "s_mov_b64 exec, %[m0]\n\t"
            "s_cbranch_execz 2f\n\t"
            LIN_LD(112:115, s0, 0, " sc1") LIN_LD(116:119, s0, 1, " sc1")
            "2:\n\t"
            "s_mov_b64 exec, %[m1]\n\t"
            "s_cbranch_execz 3f\n\t"
            LIN_LD(120:123, s1, 0, " sc1") LIN_LD(124:127, s1, 1, " sc1")
            "3:\n\t"
            "s_mov_b64 exec, %[sv]\n\t"
            "s_waitcnt vmcnt(0)\n\t"
            "s_branch 1b\n"
            "6:"
            : [sv] "=&s"(sv), [cnt] "=&s"(cnt), [m0] "=&s"(m0), [m1] "=&s"(m1), [tmp] "=&v"(tmp),
              "+{v[112:115]}"(G0), "+{v[116:119]}"(G1), "+{v[120:123]}"(G2), "+{v[124:127]}"(G3)
            : [off0] "v"(off0), [off1] "v"(off1), [s0] "s"(s0), [s1] "s"(s1), [cap] "s"(kSpinCap),
              [n0] "i"(WC == 1 ? 8 : 0)
            : "vcc", "scc", "memory");
#undef LIN_LD
#undef LIN_CHK
        u32 r[8];
#pragma unroll
        for (int i = 0; i < 4; ++i) {
            r[i] = pack_bf16(lo_f32(G0[i]) + lo_f32(G1[i]), hi_f32(G0[i]) + hi_f32(G1[i]));
            r[4 + i] = pack_bf16(lo_f32(G2[i]) + lo_f32(G3[i]), hi_f32(G2[i]) + hi_f32(G3[i]));
        }
        lds_st<u32x2>(c.kimg, u32x2{r[0], r[1]});
        lds_st<u32x2>(c.kimg + 16, u32x2{r[2], r[3]});
        lds_st<u32x4>(c.vimg, u32x4{r[4], r[5], r[6], r[7]});
    }
    if constexpr (WC == 3) {
        lds_barrier();
        return;
    }
    bf16x8 QF[8], KP[8];
    u32x4 VP[8];
    auto read_q = [&](u32 qf) {
#pragma unroll
        for (int s = 0; s < 8; ++s) QF[s] = lds_ld<bf16x8>((qf ^ ((s & 3) << 6)) + (s >> 2) * 256);
    };
    if constexpr (WC == 0) read_q(c.qfrag);
    lds_barrier();
    if constexpr (WC == 1) {
        wait_vm<0>();
        read_q(c.qfrag);
    }
    if constexpr (WC == 2) read_q(c.qfrag);
#pragma unroll
    for (int s = 0; s < 8; ++s) KP[s] = lds_ld<bf16x8>(c.fbase + s * 1024);
#pragma unroll
    for (int s = 0; s < 8; ++s) VP[s] = lds_ld<u32x4>(c.fbase + (kLdsVp - kLdsKp) + s * 1024);
    __builtin_amdgcn_sched_barrier(0);
    auto tile = [&](char* obase) {
        f32x4 sa = {0.f, 0.f, 0.f, 0.f}, sb = {0.f, 0.f, 0.f, 0.f};
#pragma unroll
        for (int s = 0; s < 8; s += 2) {
            sa = __builtin_amdgcn_mfma_f32_16x16x32_bf16(KP[s], QF[s], sa, 0, 0, 0);
            sb = __builtin_amdgcn_mfma_f32_16x16x32_bf16(KP[s + 1], QF[s + 1], sb, 0, 0, 0);
        }
        const float sc[4] = {sa[0] + sb[0], sa[1] + sb[1], sa[2] + sb[2], sa[3] + sb[3]};
        float m = fmaxf(fmaxf(sc[0], sc[1]), fmaxf(sc[2], sc[3]));
        m = xor32_max(xor16_max(m));
        float e[4];
#pragma unroll
        for (int i = 0; i < 4; ++i) e[i] = __builtin_amdgcn_exp2f((sc[i] - m) * (1.44269504088896340736f * 0.0625f));
        float l = (e[0] + e[1]) + (e[2] + e[3]);
        l = xor32_sum(xor16_sum(l));
        const float rl = __builtin_amdgcn_rcpf(l);
        u32x2 pp = {pack_bf16(e[0] * rl, e[1] * rl), pack_bf16(e[2] * rl, e[3] * rl)};
        const s16x4 P = __builtin_bit_cast(s16x4, pp);
        unroll<2>([&](auto u_) {
            constexpr int u = decltype(u_)::v;
            f32x4 D[8];
#pragma unroll
            for (int tp = 0; tp < 4; ++tp) {
                const u32x4 vv = VP[u * 4 + tp];
                const f32x4 z = {0.f, 0.f, 0.f, 0.f};
                D[2 * tp] = __builtin_amdgcn_mfma_f32_16x16x16bf16_1k(P, __builtin_bit_cast(s16x4, u32x2{vv.x, vv.y}), z, 0, 0, 0);
                D[2 * tp + 1] = __builtin_amdgcn_mfma_f32_16x16x16bf16_1k(P, __builtin_bit_cast(s16x4, u32x2{vv.z, vv.w}), z, 0, 0, 0);
            }
            unroll<4>([&](auto r_) {
                constexpr int rr = decltype(r_)::v;
                const u32x4 o = {pack_bf16(D[0][rr], D[1][rr]), pack_bf16(D[2][rr], D[3][rr]), pack_bf16(D[4][rr], D[5][rr]),
                                 pack_bf16(D[6][rr], D[7][rr])};
                store_out<rr * kRowB + u * 256>(obase, c.ooff, o);
            });
        });
    };
    if constexpr (WC == 0) {
        char* ob = c.obase;
        for (int t = 0;;) {
            tile(ob);
            if (++t >= ntile) break;
            read_q(c.qfrag2);
            ob = c.obase2;
        }
    } else {
        tile(c.obase);
    }
}

__global__ __launch_bounds__(512, 2) __attribute__((amdgpu_waves_per_eu(2, 2)))
void linformer_kernel(const char* __restrict__ gq, const char* __restrict__ gk, const char* __restrict__ gv,
                      const char* __restrict__ ge, const char* __restrict__ gf, char* __restrict__ gout,
                      char* __restrict__ gws) {
    extern __shared__ __attribute__((aligned(16))) char smem[];
    const u32 lds0 = (u32)(size_t)(LAS char*)smem;
    const int tid = threadIdx.x, b = blockIdx.x;
    const int lane = tid & 63, l15 = lane & 15, g = lane >> 4;
    const int w = __builtin_amdgcn_readfirstlane(tid >> 6);
    const int h = (b & 7) * 2 + (b >> 7);
    const int j = (b >> 3) & 15;
    const int ts = j >> 3, rh = (j >> 2) & 1, cg = j & 3;

    const u32 xo = (lane >> 3) * kRowB + (lane & 1) * 16;
    const u32 un = ((lane & 7) >> 1) ^ ((lane >> 4) & 1);
    u32 xva = xo + (un << 5), xvb = xo + ((un ^ 2) << 5) + 8 * kRowB;
    const u32 ec = (u32)(l15 ^ g) << 4;
    u32 ev0 = g * kProjB + ec, ev1 = (g + 4) * kProjB + (ec ^ 64), ev2 = (g + 8) * kProjB + (ec ^ 128),
        ev3 = (g + 12) * kProjB + (ec ^ 192);
    pin_v(xva); pin_v(xvb); pin_v(ev0); pin_v(ev1); pin_v(ev2); pin_v(ev3);
    asm volatile("" ::"s"(gq), "s"(gk), "s"(gv), "s"(ge), "s"(gf), "s"(gout), "s"(gws));
    const u64 t_start = rt();
    Ctx c;
    c.ldsw = lds0 + kLdsX + w * 16384;
    const u32 ldse = lds0 + kLdsE + w * 4096;

    const char* xbase = uni((ts ? gv : gk) + ((size_t)h * kSeq + rh * 1024 + 128 * w) * kRowB + cg * 128);
    dma_x<4>(c.ldsw, xbase, xva, xvb);
    dma_e(ldse, uni((ts ? gf : ge) + (rh * 1024 + 128 * w) * 2), ev0, ev1, ev2, ev3);
    const bool slow = cg == 3;
    const int row0 = 144 * j - 64 * (j >> 2);
    const int tile1 = slow ? (w == 5 ? 4 : w & 3) : w;
    c.qbase = uni(gq + ((size_t)h * kSeq + row0 + 16 * tile1) * kRowB);
    c.obase = uni(gout + ((size_t)h * kSeq + row0 + 16 * tile1) * kRowB);
    u32 ep, tq, to;
    asm volatile("global_load_dword %0, %1, %2 sc1" : "={v111}"(ep) : "v"(0u), "s"(uni(gws + kWsEpoch + h * 256)) : "memory");
    asm volatile("global_load_dword %0, %2, %3\n\tglobal_load_dword %1, %2, %4"
                 : "={v109}"(tq), "={v110}"(to) : "v"(0u), "s"(c.qbase), "s"(c.obase) : "memory");
    u32x4 cpf;
    {
        u32 lo, hi;
        asm volatile("s_getpc_b64 s[60:61]\n\ts_and_b32 %0, s60, 0xffffff00\n\ts_mov_b32 %1, s61" : "=s"(lo), "=s"(hi)::"s60", "s61", "scc");
        asm volatile("global_load_dwordx4 %0, %1, %3\n\tglobal_load_dwordx4 %0, %2, %3"
                     : "=&{v[104:107]}"(cpf)
                     : "v"((u32)(lane * 16 + w * 1024)), "v"((u32)(8192 + w * 128 + min(lane, 7) * 16)),
                       "s"((const char*)(((u64)hi << 32) | lo))
                     : "memory");
    }
    if (w == 0) {
        const char* dst = uni(gws + kWsPay + (h * 16 + j) * kPayB);
        const u32x4 z = {kUnwritten, kUnwritten, kUnwritten, kUnwritten};
        asm volatile("global_store_dwordx4 %0, %1, %2 sc1\n\tglobal_store_dwordx4 %0, %1, %2 offset:1024 sc1\n\ts_nop 1"
                     ::"v"((u32)(lane * 16)), "v"(z), "s"(dst) : "memory");
    }
    dma_x<12>(c.ldsw + 4096, xbase, xva, xvb);

    c.gsrc = uni(gws + kWsPay + h * 16 * kPayB + (w >> 1) * kPayB + (w & 1) * 1024);
    const char* pdst = uni(gws + kWsPay + (h * 16 + j) * kPayB + (w & 3) * 512);
    const char* fsrc = uni(gws + kWsFlag + h * 4 * 128);
    pin_s(c.qbase); pin_s(c.obase); pin_s(c.gsrc); pin_s(pdst); pin_s(fsrc);
    c.q0 = (lane >> 5) * kRowB + (((lane & 31) ^ (lane >> 5)) << 4);
    c.goff = lane * 16;
    {
        const int cgg = w >> 1, ct = 2 * (w & 1) + (lane >> 5);
        const int pl = (2 * lane) & 15, pg = (lane & 31) >> 3;
        c.kimg = lds0 + kLdsKp + (2 * cgg + (ct >> 1)) * 1024 + ((2 * (ct & 1) + (pg >> 1)) * 16 + pl) * 16 + (pg & 1) * 8;
        const int u = cgg >> 1, cc = 8 * (cgg & 1) + 2 * ct + (pl >> 3), t8 = pl & 7;
        c.vimg = lds0 + kLdsVp + (u * 4 + (t8 >> 1)) * 1024 + (pg * 16 + cc) * 16;
    }
    c.qfrag = c.ldsw + l15 * kRowB + ((g ^ l15) << 4);
    c.qfrag2 = lds0 + kLdsX + 8192 + (l15 >> 3) * 16384 + (l15 & 7) * kRowB + ((g ^ l15) << 4);
    c.obase2 = uni(gout + ((size_t)h * kSeq + row0 + 128) * kRowB);
    c.fbase = lds0 + kLdsKp + lane * 16;
    c.ooff = (4 * g) * kRowB + l15 * 16;
    u32 rbase = lds0 + kLdsX + 3 * 4096 + (w & 3) * 1024 + lane * 16;
    pin_v(c.q0); pin_v(c.goff); pin_v(c.kimg); pin_v(c.vimg); pin_v(c.qfrag); pin_v(c.fbase); pin_v(c.ooff); pin_v(rbase);
    u32 tra[4];
    {
        const u32 q4 = l15 >> 2, fl = ((q4 >> 1) & 1) | ((g & 1) << 1);
        const u32 trb = c.ldsw + (8 * g + q4) * 128 + (lane & 3) * 8;
#pragma unroll
        for (int ct = 0; ct < 4; ++ct) tra[ct] = trb + ((ct ^ fl) << 5);
    }
    const u32 era = ldse + l15 * 256 + ((g ^ l15) << 4);

    if (ts == 0) project<0>(tra, era, c.ldsw, lane);
    else project<1>(tra, era, c.ldsw, lane);
    asm volatile("" : "+{v111}"(ep) : "{v109}"(tq), "{v110}"(to));
    asm volatile("" ::"{v[104:107]}"(cpf));
    const u32 epoch = __builtin_amdgcn_readfirstlane(ep);
    const u32 T0 = (epoch + 1u) * 0x9E3779B1u;
    const u32 T1 = (epoch + 1u) * 0x85EBCA77u + 0x5bd1e995u;
    lds_barrier();

    if (w < 4) {
        f32x4 p[8];
#pragma unroll
        for (int ww = 0; ww < 4; ++ww) p[ww] = lds_ld<f32x4>(rbase + ww * 16384);
        rbase += 65536;
#pragma unroll
        for (int ww = 0; ww < 4; ++ww) p[4 + ww] = lds_ld<f32x4>(rbase + ww * 16384);
        __builtin_amdgcn_sched_barrier(0);
        const f32x4 sum = ((p[0] + p[1]) + (p[2] + p[3])) + ((p[4] + p[5]) + (p[6] + p[7]));
        const u32x2 pay = {min(pack_bf16(sum[0], sum[1]), kUnwritten - 1u), min(pack_bf16(sum[2], sum[3]), kUnwritten - 1u)};
        asm volatile("global_store_dwordx2 %0, %1, %2 sc1" ::"v"((u32)(lane * 8)), "v"(pay), "s"(pdst) : "memory");
        if (w == 0 && lane == 0)
            __hip_atomic_store((GAS u64*)(fsrc + (j >> 2) * 128 + cg * 8), ((u64)T1 << 32) | T0, __ATOMIC_RELAXED,
                               __HIP_MEMORY_SCOPE_AGENT);
    }
    if (w < kEarlyQ && w != kPoller) {
        while (rt() - t_start < kEarlyQTicks) __builtin_amdgcn_s_sleep(1);
        dma_q(c.ldsw, c.qbase, c.q0);
        if (w == kPoller + 1 && !slow) dma_q(c.ldsw - 16384, c.qbase - 16 * kRowB, c.q0);
        if (w == 0 && !slow) dma_q(lds0 + kLdsX + 8192, c.qbase + 128 * kRowB, c.q0, 16384);
    } else if (w == kPoller) {
        const u64 tag = ((u64)T1 << 32) | T0;
        u64 miss;
        u32 polls;
        asm volatile(
            "s_mov_b32 %[n], 0\n"
            "1:\n\t"
            "s_load_dwordx8 s[64:71], %[base], 0x0 glc\n\t"
            "s_load_dwordx8 s[72:79], %[base], 0x80 glc\n\t"
            "s_load_dwordx8 s[80:87], %[base], 0x100 glc\n\t"
            "s_load_dwordx8 s[88:95], %[base], 0x180 glc\n\t"
            "s_waitcnt lgkmcnt(0)\n\t"
            "s_xor_b64 %[miss], s[64:65], %[tag]\n\t"
#define LIN_SX(A) "s_xor_b64 vcc, s[" #A "], %[tag]\n\ts_or_b64 %[miss], %[miss], vcc\n\t"
            LIN_SX(66:67) LIN_SX(68:69) LIN_SX(70:71) LIN_SX(72:73) LIN_SX(74:75) LIN_SX(76:77) LIN_SX(78:79)
            LIN_SX(80:81) LIN_SX(82:83) LIN_SX(84:85) LIN_SX(86:87) LIN_SX(88:89) LIN_SX(90:91) LIN_SX(92:93)
            LIN_SX(94:95)
#undef LIN_SX
            "s_cbranch_scc0 2f\n\t"
            "s_add_u32 %[n], %[n], 1\n\t"
            "s_cmp_gt_u32 %[n], %[cap]\n\t"
            "s_cbranch_scc1 2f\n\t"
            "s_sleep 1\n\t"
            "s_branch 1b\n"
            "2:"
            : [miss] "=&s"(miss), [n] "=&s"(polls)
            : [base] "s"(fsrc), [tag] "s"(tag), [cap] "s"(kScalarPolls)
            : "vcc", "scc", "memory", "s64", "s65", "s66", "s67", "s68", "s69", "s70", "s71", "s72", "s73", "s74", "s75",
              "s76", "s77", "s78", "s79", "s80", "s81", "s82", "s83", "s84", "s85", "s86", "s87", "s88", "s89", "s90",
              "s91", "s92", "s93", "s94", "s95");
        if (miss != 0) {
            const u32 off = (l15 >> 2) * 128 + (l15 & 3) * 8;
            for (u32 n = 0;;) {
                u32x2 v;
                asm volatile("global_load_dwordx2 %0, %1, %2 sc1\n\ts_waitcnt vmcnt(0)" : "=&v"(v) : "v"(off), "s"(fsrc) : "memory");
                if (__builtin_amdgcn_ballot_w64((v.x != T0) | (v.y != T1)) == 0 || ++n > kSpinCap) break;
                __builtin_amdgcn_s_sleep(1);
            }
        }
    }
    raw_barrier();
    if (j == 0 && w == 7 && lane == 0)
        __hip_atomic_store((GAS u32*)(gws + kWsEpoch + h * 256), epoch + 1u, __ATOMIC_RELAXED, __HIP_MEMORY_SCOPE_AGENT);

    if (slow && (w == kPoller || w >= kEarlyQ)) attend<3>(c, 0);
    else if (w == kPoller) attend<2>(c, 1);
    else if (w < kEarlyQ) attend<0>(c, (w == 0 && !slow) ? 2 : 1);
    else attend<1>(c, 1);
}

extern "C" void kernel_launch(void* const* d_in, const int* in_sizes, int n_in, void* d_out, int out_size, void* d_ws,
                              size_t ws_size, hipStream_t stream) {
    const char* q = (const char*)d_in[0];
    const char* k = (const char*)d_in[1];
    const char* v = (const char*)d_in[2];
    const char* e = (const char*)d_in[3];
    const char* f = (const char*)d_in[4];
    (void)hipFuncSetAttribute((const void*)linformer_kernel, hipFuncAttributeMaxDynamicSharedMemorySize, kLdsBytes);
    linformer_kernel<<<256, 512, kLdsBytes, stream>>>(q, k, v, e, f, (char*)d_out, (char*)d_ws);
}
```
